```python
import torch

def _softmax(x, axis=-1):
    return torch.softmax(x, dim=axis)
def _gelu(x):
    return torch.nn.functional.gelu(x, approximate="tanh")
def _silu(x):
    return torch.nn.functional.silu(x)
def _sigmoid(x):
    return torch.sigmoid(x)
def _layernorm(x, axis=-1, eps=1e-5):
    m = x.mean(dim=axis, keepdim=True)
    v = x.var(dim=axis, keepdim=True, unbiased=False)
    return (x - m) / torch.sqrt(v + eps)
def _scatter_add(tgt, idx, src, axis=0):
    return tgt.clone().index_add_(axis, idx, src)
def _segment_sum(src, seg, n):
    out = torch.zeros((n,) + src.shape[1:], dtype=src.dtype)
    return out.index_add_(0, seg, src)
def _pad(x, pw):
    flat = tuple(v for pair in reversed(pw) for v in pair)
    return torch.nn.functional.pad(x, flat)

kernel_name = 'gated_mix__sliding_block_cached__glu_gated_reglu__decode'

def setup_inputs():

    _w = (lambda a: (lambda t: t.to(torch.bfloat16) if t.is_floating_point() else t)(torch.as_tensor(a)))
    def _A_setup():
        B, Sq, D = 1, 1, 640
        Hq, Hkv, Sk, Dh = 32, 32, 32768, 192
        assert Hq % Hkv == 0, (Hq, Hkv)
        return {
            "x": _w((lambda *s: torch.randn(*s, dtype=torch.bfloat16) * 0.05)(B, Sq, D)),
            "k_cache": _w((lambda *s: torch.randn(*s, dtype=torch.bfloat16) * 6.4)(B, Hkv, Sk, Dh)),
            "v_cache": _w((lambda *s: torch.randn(*s, dtype=torch.bfloat16) * 0.05)(B, Hkv, Sk, Dh)),
            "wq": _w((lambda *s: torch.randn(*s, dtype=torch.bfloat16) * 6.4)(D, Hq * Dh) * (D ** -0.5)),
            "wo": _w((lambda *s: torch.randn(*s, dtype=torch.bfloat16) * 0.05)(Hq * Dh, D) * ((Hq * Dh) ** -0.5)),
        }
    _A_in = _A_setup()

    def _B_setup():
        B, S, D, F = 1, 1, 640, 1536
        return {
            "x": _w((lambda *s: torch.randn(*s, dtype=torch.bfloat16) * 0.05)(B, S, D)),
            "W_gate": _w((lambda *s: torch.randn(*s, dtype=torch.bfloat16) * 0.4)(D, F) * (2.0 / D) ** 0.5),
            "W_up": _w((lambda *s: torch.randn(*s, dtype=torch.bfloat16) * 0.4)(D, F) * (2.0 / D) ** 0.5),
            "W_down": _w((lambda *s: torch.randn(*s, dtype=torch.bfloat16) * 0.4)(F, D) * (1.0 / F) ** 0.5),
        }
    _B_in = _B_setup()

    return {
        "A_x": _A_in["x"],
        "A_k_cache": _A_in["k_cache"],
        "A_v_cache": _A_in["v_cache"],
        "A_wq": _A_in["wq"],
        "A_wo": _A_in["wo"],
        "B_x": _B_in["x"],
        "B_W_gate": _B_in["W_gate"],
        "B_W_up": _B_in["W_up"],
        "B_W_down": _B_in["W_down"],
        "gate_w": _w((lambda *s: torch.randn(*s, dtype=torch.bfloat16) * 0.1)(640) * 16.0),
    }

def reference(A_x, A_k_cache, A_v_cache, A_wq, A_wo, B_x, B_W_gate, B_W_up, B_W_down, gate_w):
    def _A_ref(x, k_cache, v_cache, wq, wo):
        B, Sq, D = x.shape
        Hq, Dh = 32, 192
        Hkv, Sk = k_cache.shape[1], k_cache.shape[2]
        W = min(4096, max(256, Sk // 4))
        rep = Hq // Hkv
        q = (lambda x, *d: x.permute(*d))((x @ wq).reshape(B, Sq, Hq, Dh), 0, 2, 1, 3)
        k = (lambda x, n, axis: torch.repeat_interleave(x, n, dim=axis))(k_cache[:, :, Sk - W:, :], rep, axis=1)
        v = (lambda x, n, axis: torch.repeat_interleave(x, n, dim=axis))(v_cache[:, :, Sk - W:, :], rep, axis=1)
        scores = torch.einsum("bhqd,bhkd->bhqk", q, k) * (Dh ** -0.5)
        p = _softmax(scores, axis=-1)
        o = torch.einsum("bhqk,bhkd->bhqd", p, v)
        return (lambda x, *d: x.permute(*d))(o, 0, 2, 1, 3).reshape(B, Sq, Hq * Dh) @ wo

    def _B_ref(x, W_gate, W_up, W_down):
        gate = torch.einsum("bsd,df->bsf", x, W_gate)
        up = torch.einsum("bsd,df->bsf", x, W_up)
        gate = gate * (lambda x, dt: x.to(dt))(gate > 0, gate.dtype)
        h = gate * up
        return torch.einsum("bsf,fd->bsd", h, W_down)

    x = A_x
    g = _sigmoid((x * gate_w).sum(-1, keepdims=True))
    a = _A_ref(x, A_k_cache, A_v_cache, A_wq, A_wo)
    b = _B_ref(x, B_W_gate, B_W_up, B_W_down)
    return g * a + (1 - g) * b
```

```cpp
#include <hip/hip_runtime.h>
#include <hip/hip_bf16.h>
#include <stdint.h>

#define GAS __attribute__((address_space(1)))
typedef unsigned v4u __attribute__((ext_vector_type(4)));
typedef unsigned v2u __attribute__((ext_vector_type(2)));
typedef float f32x4 __attribute__((ext_vector_type(4)));
typedef float f32x2 __attribute__((ext_vector_type(2)));
typedef __bf16 bf16x2 __attribute__((ext_vector_type(2)));
typedef unsigned short u16;
typedef unsigned long long u64;

constexpr int D = 640, HQ = 32, DH = 192, SK = 32768, WIN = 4096, F = 1536, NQ = HQ * DH;
constexpr int NWG = 256, NTHR = 1024;
constexpr int WS_EPOCH = 0;
constexpr int WS_SLOT = 4096;
constexpr int S_IP = 0, S_O = 512, S_FIN = 1536, S_SIZE = 2816;
constexpr int WS_END = WS_SLOT + 256 * S_SIZE;
constexpr int AUX_SC1 = 16;
constexpr int OOB = 0x7FFFFFF0;
constexpr unsigned SPIN_CAP = 1u << 18;
constexpr unsigned TM = 0x3FFu;

__device__ __forceinline__ float bflo(unsigned u) { return __builtin_bit_cast(float, u << 16); }
__device__ __forceinline__ float bfhi(unsigned u) { return __builtin_bit_cast(float, u & 0xffff0000u); }
__device__ __forceinline__ v4u ldnt(const void* p) { return __builtin_nontemporal_load((const GAS v4u*)p); }
__device__ __forceinline__ float dot2(unsigned k, unsigned q, float acc) {
  return __builtin_amdgcn_fdot2_f32_bf16(__builtin_bit_cast(bf16x2, k), __builtin_bit_cast(bf16x2, q), acc, false);
}
template <int CTRL> __device__ __forceinline__ float dpp(float x) {
  return __builtin_bit_cast(float, __builtin_amdgcn_mov_dpp(__builtin_bit_cast(int, x), CTRL, 0xf, 0xf, true));
}
constexpr int XOR1 = 0xB1, XOR2 = 0x4E, HMIR = 0x141, ROR8 = 0x128, ROR4 = 0x124, ROR2 = 0x122, ROR1 = 0x121;
__device__ __forceinline__ float tr32(float a, float b) {
  auto r = __builtin_amdgcn_permlane32_swap(__float_as_uint(a), __float_as_uint(b), false, false);
  return __uint_as_float(r[0]) + __uint_as_float(r[1]);
}
__device__ __forceinline__ float tr16(float a, float b) {
  auto r = __builtin_amdgcn_permlane16_swap(__float_as_uint(a), __float_as_uint(b), false, false);
  return __uint_as_float(r[0]) + __uint_as_float(r[1]);
}
__device__ __forceinline__ float tr8(float a, float b, bool hi8) {
  const float keep = hi8 ? b : a, send = hi8 ? a : b;
  return keep + dpp<ROR8>(send);
}
__device__ __forceinline__ float red8(const float* v, bool hi8) {
  const float a0 = tr32(v[0], v[4]), a1 = tr32(v[1], v[5]), a2 = tr32(v[2], v[6]), a3 = tr32(v[3], v[7]);
  const float b0 = tr16(a0, a2), b1 = tr16(a1, a3);
  return tr8(b0, b1, hi8);
}
__device__ __forceinline__ float sum8(float x) { x += dpp<XOR1>(x); x += dpp<XOR2>(x); x += dpp<HMIR>(x); return x; }
__device__ __forceinline__ float maxG(float x) {
  x = fmaxf(x, dpp<ROR8>(x));
  auto s = __builtin_amdgcn_permlane16_swap(__float_as_uint(x), __float_as_uint(x), false, false);
  x = fmaxf(__uint_as_float(s[0]), __uint_as_float(s[1]));
  auto t = __builtin_amdgcn_permlane32_swap(__float_as_uint(x), __float_as_uint(x), false, false);
  return fmaxf(__uint_as_float(t[0]), __uint_as_float(t[1]));
}
__device__ __forceinline__ float sumG(float x) { x += dpp<ROR8>(x); x = tr16(x, x); return tr32(x, x); }
__device__ __forceinline__ unsigned enc(float v, unsigned t) { return (__float_as_uint(v) & ~TM) | t; }
__device__ __forceinline__ float dec(unsigned u) { return __uint_as_float(u & ~TM); }
#define VMWAIT(n) asm volatile("s_waitcnt vmcnt(%0)" ::"n"(n) : "memory")
#define LGKM0() asm volatile("s_waitcnt lgkmcnt(0)" ::: "memory")
#define CBAR() asm volatile("" ::: "memory")
#define SCHED() __builtin_amdgcn_sched_barrier(0)

constexpr int RS = 260;
constexpr int PS = 324;
constexpr int L_RED = 0;
constexpr int L_Q = 16 * PS;
constexpr int L_ML = L_Q + 96;
constexpr int L_COEF = L_ML + 32;
constexpr int L_PART = L_COEF + 64;
constexpr int L_TAG = L_PART + 128;
constexpr int L_CNT = L_TAG + 4;
enum { C_P0 = 0, C_Q = 1, C_ML = 2, C_PROG = 4 };
constexpr int L_END = L_CNT + 8;

__device__ __forceinline__ void lds_arrive(float* lds, int c, int lane) {
  LGKM0();
  if (lane == 0) __hip_atomic_fetch_add((unsigned*)(lds + L_CNT) + c, 1u, __ATOMIC_RELAXED, __HIP_MEMORY_SCOPE_WORKGROUP);
  CBAR();
}
__device__ __forceinline__ void lds_wait(float* lds, int c, unsigned n) {
  while (__builtin_amdgcn_readfirstlane(__hip_atomic_load((unsigned*)(lds + L_CNT) + c, __ATOMIC_RELAXED, __HIP_MEMORY_SCOPE_WORKGROUP)) < n)
    __builtin_amdgcn_s_sleep(1);
  CBAR();
}

__global__ void __launch_bounds__(NTHR, 4) fused_kernel(const u16* __restrict__ x, const u16* __restrict__ kc, const u16* __restrict__ vc,
                                                     const u16* __restrict__ wq, const u16* __restrict__ wo, const u16* __restrict__ wg,
                                                     const u16* __restrict__ wu, const u16* __restrict__ wd, const u16* __restrict__ gw,
                                                     u16* __restrict__ out, char* ws) {
  __shared__ __attribute__((aligned(16))) float lds[L_END];
  const int b = blockIdx.x, grp = b & 7, idx = b >> 3;
  const int h = grp * 4 + (idx >> 3), s = idx & 7;
  const int fi = grp * 32 + idx;
  const int rg = s >> 1, ch = s & 1;
  const int tid = threadIdx.x, lane = tid & 63, wave = __builtin_amdgcn_readfirstlane(tid >> 6);
  const int G = lane >> 3, j = lane & 7;
  const bool hi8 = (lane & 8) != 0;
  const __amdgpu_buffer_rsrc_t rs = __builtin_amdgcn_make_buffer_rsrc((void*)ws, 0, WS_END, 0x00020000);
  const char* kb = (const char*)kc + ((size_t)h * SK + (SK - WIN) + s * 512 + wave * 32) * (DH * 2) + G * 384 + j * 16;
  const char* vb = (const char*)vc + ((size_t)h * SK + (SK - WIN) + s * 512 + wave * 32) * (DH * 2) + G * 384 + j * 16;
#define LDP(voff, soff) __builtin_amdgcn_raw_buffer_load_b128(rs, (voff), (soff), AUX_SC1)
#define STP(a, c, d, e, off) __builtin_amdgcn_raw_buffer_store_b128(v4u{enc(a, t0_), enc(c, t1_), enc(d, t2_), enc(e, t3_)}, rs, (off), 0, AUX_SC1)
#define BAD(t) ((((t).x ^ t0_) | ((t).y ^ t1_) | ((t).z ^ t2_) | ((t).w ^ t3_)) & TM)
#define SET_TAGS(lo, hi) do { t0_ = (lo) & TM; t1_ = ((lo) >> 16) & TM; t2_ = (hi) & TM; t3_ = ((hi) >> 16) & TM; } while (0)
  const int slot = WS_SLOT + fi * S_SIZE;

  v4u kr[12];
  asm volatile("" ::"s"(x), "s"(vc), "s"(wq), "s"(wo), "s"(wg), "s"(wu), "s"(wd), "s"(gw), "s"(out), "s"(ws));
  kr[0] = ldnt(kb);
  kr[1] = ldnt(kb + 128);
  CBAR(); SCHED();
  v4u w0, w1, w2;
  unsigned xr = 0;
  u64 tag = 0;
  if (wave < 15) {
    const char* wp[3];
#pragma unroll
    for (int t = 0; t < 3; ++t) {
      const unsigned pos = 360u * idx + 24u * wave + 8u * t, c = pos / 640u, row0 = pos - c * 640u;
      const bool isq = c < 12u;
      const unsigned stride = isq ? NQ * 2u : F * 2u;
      const unsigned coff = isq ? grp * 1536u + c * 128u : grp * 384u + (c < 15u ? c - 12u : c - 15u) * 128u;
      const char* base = (const char*)(isq ? wq : (c < 15u ? wg : wu)) + (row0 * stride + coff);
      wp[t] = base + (unsigned)(G * stride + j * 16);
    }
    unsigned xi = (360u * idx + 24u * wave) % 640u + (lane < 24 ? lane : 0);
    xi = xi >= 640u ? xi - 640u : xi;
    xr = x[xi];
    CBAR();
    w0 = ldnt(wp[0]); w1 = ldnt(wp[1]); w2 = ldnt(wp[2]);
  }
  CBAR(); SCHED();
  v4u g0, g1, g2;
  if (wave == 15) {
    tag = __hip_atomic_load((GAS u64*)(ws + WS_EPOCH), __ATOMIC_RELAXED, __HIP_MEMORY_SCOPE_AGENT);
    g0 = *(const GAS v4u*)(x + lane * 8);
    g1 = *(const GAS v4u*)(gw + lane * 8);
    g2 = *(const GAS v4u*)(((lane & 16) ? gw : x) + 512 + (lane & 15) * 8);
    if (lane < 8) ((unsigned*)(lds + L_CNT))[lane] = 0u;
  }
  CBAR();
  __syncthreads();
  bool ok = true;
  float gmix = 0.f;
  unsigned t0_ = 0, t1_ = 0, t2_ = 0, t3_ = 0;
  int vo[3] = {OOB, OOB, OOB}, fo = OOB;
  VMWAIT(4);
  kr[2] = ldnt(kb + 256); CBAR(); SCHED();
  VMWAIT(4);
  kr[3] = ldnt(kb + 3072); CBAR(); SCHED();
  asm volatile("" : "+v"(xr), "+v"(tag));
  if (wave < 15) {
    float* p0dst = lds + L_RED + wave * 192 + j * 8 + G;
    float p[8];
    float xv = bflo(__builtin_amdgcn_ds_bpermute(G * 4, xr));
#pragma unroll
    for (int c = 0; c < 4; ++c) { p[2 * c] = xv * bflo(w0[c]); p[2 * c + 1] = xv * bfhi(w0[c]); }
    p0dst[0] = red8(p, hi8);
    xv = bflo(__builtin_amdgcn_ds_bpermute(32 + G * 4, xr));
#pragma unroll
    for (int c = 0; c < 4; ++c) { p[2 * c] = xv * bflo(w1[c]); p[2 * c + 1] = xv * bfhi(w1[c]); }
    p0dst[64] = red8(p, hi8);
    xv = bflo(__builtin_amdgcn_ds_bpermute(64 + G * 4, xr));
#pragma unroll
    for (int c = 0; c < 4; ++c) { p[2 * c] = xv * bflo(w2[c]); p[2 * c + 1] = xv * bfhi(w2[c]); }
    p0dst[128] = red8(p, hi8);
    lds_arrive(lds, C_P0, lane);
  } else {
    tag = ((tag & 0x7FFF7FFF7FFF7FFFull) + 0x0001000100010001ull);
    if (lane == 0) *(u64*)(lds + L_TAG) = tag;
    const unsigned lo = __builtin_amdgcn_readfirstlane((unsigned)tag), hi = __builtin_amdgcn_readfirstlane((unsigned)(tag >> 32));
    SET_TAGS(lo, hi);
    float z = 0.f;
#pragma unroll
    for (int c = 0; c < 4; ++c) z = dot2(g0[c], g1[c], z);
    {
      v4u o;
#pragma unroll
      for (int c = 0; c < 4; ++c) o[c] = __builtin_amdgcn_permlane16_swap(g2[c], g2[c], false, false)[1];
      if (lane < 16) {
#pragma unroll
        for (int c = 0; c < 4; ++c) z = dot2(g2[c], o[c], z);
      }
    }
    z = sumG(sum8(z));
    gmix = 1.f / (1.f + __expf(-z));
    {
      const int c = 3 * (idx >> 3) + (lane >> 4), k0 = (640 * c) / 360, k1 = (640 * c + 639) / 360;
#pragma unroll
      for (int n = 0; n < 3; ++n) {
        const int k = k0 + n;
        vo[n] = (lane < 48 && k <= k1) ? k * S_SIZE + (c == (360 * k) / 640 ? 0 : 256) + (lane & 15) * 16 : OOB;
      }
    }
    {
      const int n = (lane >> 3) & 3, cq = (idx >> 1) * 3 + j, c = 12 + 3 * (lane >> 5) + (cq >> 4);
      const int k = (640 * c) / 360 + n, k1 = (640 * c + 639) / 360;
      fo = (j < 3 && k <= k1) ? k * S_SIZE + (c == (360 * k) / 640 ? 0 : 256) + (cq & 15) * 16 : OOB;
    }
    asm volatile("" : "+v"(vo[0]), "+v"(vo[1]), "+v"(vo[2]), "+v"(fo));
    const int ls = (640 * ((360 * idx) / 640 + 1) - 360 * idx) / 8;
    lds_wait(lds, C_P0, 15);
    f32x4 a = {0.f, 0.f, 0.f, 0.f}, c = {0.f, 0.f, 0.f, 0.f};
#pragma unroll
    for (int i = 0; i < 12; ++i) {
      const int L = 4 * i + (lane >> 4);
      const f32x4 v = *(const f32x4*)(lds + L_RED + (L < 45 ? L : 0) * 64 + (lane & 15) * 4);
      const f32x4 z4 = {0.f, 0.f, 0.f, 0.f};
      a += L < ls && L < 45 ? v : z4;
      c += L >= ls && L < 45 ? v : z4;
    }
    float u0 = tr32(a.x, c.x), u1 = tr32(a.y, c.y), u2 = tr32(a.z, c.z), u3 = tr32(a.w, c.w);
    u0 = tr16(u0, u0); u1 = tr16(u1, u1); u2 = tr16(u2, u2); u3 = tr16(u3, u3);
    STP(u0, u1, u2, u3, !(lane & 16) && (lane < 32 || ls < 45) ? slot + S_IP + (lane >> 5) * 256 + (lane & 15) * 16 : OOB);
  }
  CBAR();
#define K_LOAD(i) do { if (wave != 15) VMWAIT(1); else lds_wait(lds, C_PROG, (i)); \
    kr[i] = ldnt(kb + ((i) / 3) * 3072 + ((i) % 3) * 128); CBAR(); \
    if (wave == 0) __hip_atomic_store((unsigned*)(lds + L_CNT) + C_PROG, (unsigned)(i), __ATOMIC_RELAXED, __HIP_MEMORY_SCOPE_WORKGROUP); \
    SCHED(); } while (0)
  K_LOAD(4);
  K_LOAD(5);
  K_LOAD(6);
  K_LOAD(7);
  K_LOAD(8);
  if (wave == 15) {
    const int so = WS_SLOT + grp * 32 * S_SIZE + S_IP;
    v4u t[3], tf;
#pragma unroll
    for (int n = 0; n < 3; ++n) t[n] = LDP(vo[n], so);
    tf = LDP(fo, so);
    CBAR(); SCHED();
    K_LOAD(9);
    K_LOAD(10);
#pragma nounroll
    for (unsigned spins = 0;; ++spins) {
      unsigned bad = 0;
#pragma unroll
      for (int n = 0; n < 3; ++n) bad |= vo[n] != OOB ? BAD(t[n]) : 0u;
      bad |= fo != OOB ? BAD(tf) : 0u;
      if (bad == 0) break;
      if (spins > SPIN_CAP) { ok = false; break; }
      __builtin_amdgcn_s_sleep(4);
      CBAR();
#pragma unroll
      for (int n = 0; n < 3; ++n) t[n] = LDP(vo[n], so);
      tf = LDP(fo, so);
    }
    f32x4 a = {0.f, 0.f, 0.f, 0.f};
#pragma unroll
    for (int n = 0; n < 3; ++n) a += f32x4{dec(t[n].x), dec(t[n].y), dec(t[n].z), dec(t[n].w)};
    const bf16x2 lo = {(__bf16)a.x, (__bf16)a.y}, hi = {(__bf16)a.z, (__bf16)a.w};
    const v2u pk = {__builtin_bit_cast(unsigned, lo), __builtin_bit_cast(unsigned, hi)};
    if (lane < 48) *(v2u*)(lds + L_Q + lane * 2) = pk;
    lds_arrive(lds, C_Q, lane);
    f32x4 hv;
#pragma unroll
    for (int e = 0; e < 4; ++e) {
      float v = dec(tf[e]);
      v += dpp<ROR8>(v);
      v = tr16(v, v);
      auto r = __builtin_amdgcn_permlane32_swap(__float_as_uint(v), __float_as_uint(v), false, false);
      const float ga = __uint_as_float(r[0]), up = __uint_as_float(r[1]);
      hv[e] = ga * (ga > 0.f ? 1.f : 0.f) * up * (1.f - gmix);
    }
    if (lane < 3) *(f32x4*)(lds + L_COEF + 48 + lane * 4) = hv;
  } else {
    K_LOAD(9);
    K_LOAD(10);
  }
  CBAR();
  K_LOAD(11);
  v4u vr[12];
#define V_LOADU(i) do { vr[i] = ldnt(vb + ((i) / 3) * 3072 + ((i) % 3) * 128); CBAR(); SCHED(); } while (0)
#define V_LOAD(i) do { VMWAIT(1); V_LOADU(i); } while (0)
#define V_LOAD0(i) do { if (wave != 15) VMWAIT(1); else VMWAIT(3); V_LOADU(i); } while (0)
  V_LOAD0(0); V_LOAD0(1); V_LOAD0(2); V_LOAD(3);
  lds_wait(lds, C_Q, 1);
  const unsigned tlo = __builtin_amdgcn_readfirstlane(((const unsigned*)(lds + L_TAG))[0]);
  const unsigned thi = __builtin_amdgcn_readfirstlane(((const unsigned*)(lds + L_TAG))[1]);
  SET_TAGS(tlo, thi);
  float pr[4];
  float mw;
  {
    const v4u q0 = *(const v4u*)(lds + L_Q + j * 4), q1 = *(const v4u*)(lds + L_Q + 32 + j * 4), q2 = *(const v4u*)(lds + L_Q + 64 + j * 4);
#pragma unroll
    for (int i = 0; i < 4; ++i) {
      float d0 = 0.f, d1 = 0.f, d2 = 0.f;
#pragma unroll
      for (int c = 0; c < 4; ++c) { d0 = dot2(kr[3 * i][c], q0[c], d0); d1 = dot2(kr[3 * i + 1][c], q1[c], d1); d2 = dot2(kr[3 * i + 2][c], q2[c], d2); }
      pr[i] = (float)(__bf16)((float)(__bf16)sum8(d0 + d1 + d2) * 0.07216878364870322f) * 1.4426950408889634f;
    }
    mw = maxG(fmaxf(fmaxf(pr[0], pr[1]), fmaxf(pr[2], pr[3])));
#pragma unroll
    for (int i = 0; i < 4; ++i) pr[i] = __builtin_amdgcn_exp2f(pr[i] - mw);
    const float lw = sumG((pr[0] + pr[1]) + (pr[2] + pr[3]));
    if (lane == 0) *(f32x2*)(lds + L_ML + wave * 2) = f32x2{mw, lw};
    lds_arrive(lds, C_ML, lane);
  }
  float acc[24];
#pragma unroll
  for (int i = 0; i < 24; ++i) acc[i] = 0.f;
  float scale = 0.f, lse = 0.f;
#define PV_STEP(i) do { const float p_ = pr[(i) / 3]; constexpr int t_ = (i) % 3; \
    asm volatile("" : "+v"(vr[i]) :: "memory"); \
    _Pragma("unroll") for (int c = 0; c < 4; ++c) { \
      acc[t_ * 8 + 2 * c] = fmaf(p_, bflo(vr[i][c]), acc[t_ * 8 + 2 * c]); \
      acc[t_ * 8 + 2 * c + 1] = fmaf(p_, bfhi(vr[i][c]), acc[t_ * 8 + 2 * c + 1]); } \
    asm volatile("" : "+v"(acc[t_ * 8]), "+v"(acc[t_ * 8 + 1]), "+v"(acc[t_ * 8 + 2]), "+v"(acc[t_ * 8 + 3]), "+v"(acc[t_ * 8 + 4]), \
                 "+v"(acc[t_ * 8 + 5]), "+v"(acc[t_ * 8 + 6]), "+v"(acc[t_ * 8 + 7]) :: "memory"); \
    SCHED(); } while (0)
#define O_RED(t) do { ored[t] = red8(acc + (t) * 8, hi8) * scale; CBAR(); } while (0)
  float* ored = lds + L_RED + wave * RS + (j * 8 + G) * 4;
  V_LOAD(4); PV_STEP(0); PV_STEP(1);
  V_LOAD(5); PV_STEP(2);
  V_LOAD(6); PV_STEP(3);
  V_LOAD(7); PV_STEP(4);
  V_LOAD(8); PV_STEP(5);
  {
    lds_wait(lds, C_ML, 16);
    const f32x2 ml = *(const f32x2*)(lds + L_ML + (lane & 15) * 2);
    float M = ml.x;
    M = fmaxf(M, dpp<ROR8>(M)); M = fmaxf(M, dpp<ROR4>(M)); M = fmaxf(M, dpp<ROR2>(M)); M = fmaxf(M, dpp<ROR1>(M));
    float L = ml.y * __builtin_amdgcn_exp2f(ml.x - M);
    L += dpp<ROR8>(L); L += dpp<HMIR>(L); L += dpp<XOR2>(L); L += dpp<XOR1>(L);
    scale = __builtin_amdgcn_exp2f(mw - M) / L;
    lse = M + __builtin_amdgcn_logf(L);
    const float lh = dec(__float_as_uint(lse));
    lse = (lane & 4) ? lse - lh : lh;
    asm volatile("" : "+v"(scale), "+v"(lse));
  }
  V_LOAD(9); V_LOADU(10); V_LOADU(11);
  PV_STEP(6); PV_STEP(7); PV_STEP(8);
  int csrc_ = L_RED + (tid & 3) * RS + ((tid >> 2) & 63) * 4;
  int o_off = (tid & 3) == 0 && wave < 4 ? slot + S_O + tid * 4 : OOB;
  int cfp_ = L_COEF + wave * 4;
  int pdst_ = L_RED + wave * PS + lane * 4;
  const int cq3 = tid >> 2;
  int psrc_ = L_RED + (tid & 3) * PS + (cq3 & 1) * 160 + ((cq3 >> 1) % 40) * 4;
  int f_off = (tid & 3) == 0 ? slot + S_FIN + tid * 4 : OOB;
  csrc_ *= 4; cfp_ *= 4; pdst_ *= 4; psrc_ *= 4;
  asm volatile("" : "+v"(csrc_), "+v"(o_off), "+v"(cfp_), "+v"(pdst_), "+v"(psrc_), "+v"(f_off));
  const float* csrc = (const float*)((const char*)lds + csrc_);
  const float* cfp = (const float*)((const char*)lds + cfp_);
  float* pdst = (float*)((char*)lds + pdst_);
  const float* psrc = (const float*)((const char*)lds + psrc_);
  f32x2 wf[16];
  {
    const __amdgpu_buffer_rsrc_t rw = __builtin_amdgcn_make_buffer_rsrc((void*)(wave < 12 ? wo : wd), 0, (wave < 12 ? NQ : F) * D * 2, 0x00020000);
    int ro[4];
#pragma unroll
    for (int i = 0; i < 4; ++i) {
      const int r = wave * 4 + i, jj = r / 24, gp = (r % 24) / 3, t = r % 3;
      ro[i] = (wave < 12 ? h * DH + t * 64 + (2 * rg + jj) * 8 + gp : grp * 192 + (idx >> 1) * 12 + (wave < 15 ? r - 48 : 0)) * (D * 2) + ch * 640;
    }
    const int wvo = lane < 40 && wave < 15 ? lane * 16 : OOB;
    PV_STEP(9); O_RED(0);
    PV_STEP(10); O_RED(1);
    asm volatile("" : "+v"(vr[11]) :: "memory");
    v4u wr0, wr1, wr2, wr3;
    asm volatile("buffer_load_dwordx4 %0, %4, %5, %6 offen nt\n\tbuffer_load_dwordx4 %1, %4, %5, %7 offen nt\n\t"
                 "buffer_load_dwordx4 %2, %4, %5, %8 offen nt\n\tbuffer_load_dwordx4 %3, %4, %5, %9 offen nt"
                 : "=&v"(wr0), "=&v"(wr1), "=&v"(wr2), "=&v"(wr3)
                 : "v"(wvo), "s"(rw), "s"(ro[0]), "s"(ro[1]), "s"(ro[2]), "s"(ro[3])
                 : "memory");
    PV_STEP(11); O_RED(2);
    __syncthreads();
    if (wave < 4) {
      f32x4 a = (*(const f32x4*)csrc + *(const f32x4*)(csrc + 4 * RS)) + (*(const f32x4*)(csrc + 8 * RS) + *(const f32x4*)(csrc + 12 * RS));
      a.x += dpp<XOR1>(a.x); a.y += dpp<XOR1>(a.y); a.z += dpp<XOR1>(a.z);
      a.x += dpp<XOR2>(a.x); a.y += dpp<XOR2>(a.y); a.z += dpp<XOR2>(a.z);
      STP(a.x, a.y, a.z, lse, o_off);
      VMWAIT(1);
    } else {
      VMWAIT(0);
    }
    asm volatile("" : "+v"(wr0), "+v"(wr1), "+v"(wr2), "+v"(wr3) :: "memory");
#pragma unroll
    for (int c = 0; c < 4; ++c) {
      wf[c] = f32x2{bflo(wr0[c]), bfhi(wr0[c])};
      wf[4 + c] = f32x2{bflo(wr1[c]), bfhi(wr1[c])};
      wf[8 + c] = f32x2{bflo(wr2[c]), bfhi(wr2[c])};
      wf[12 + c] = f32x2{bflo(wr3[c]), bfhi(wr3[c])};
    }
#pragma unroll
    for (int k = 0; k < 16; k += 4) asm volatile("" : "+v"(wf[k]), "+v"(wf[k + 1]), "+v"(wf[k + 2]), "+v"(wf[k + 3]));
  }
#define CHK4(T, U, A, B, C, E) "v_xor_b32 " T ", %[t0], " A "\n\tv_xor_b32 " U ", %[t1], " B "\n\tv_or_b32 " T ", " T ", " U "\n\t" \
                               "v_xor_b32 " U ", %[t2], " C "\n\tv_or_b32 " T ", " T ", " U "\n\t" \
                               "v_xor_b32 " U ", %[t3], " E "\n\tv_or_b32 " T ", " T ", " U "\n\t"
#define MOV4(R0, R1, R2, R3, A, B, C, E) "v_mov_b32 " R0 ", " A "\n\tv_mov_b32 " R1 ", " B "\n\tv_mov_b32 " R2 ", " C "\n\tv_mov_b32 " R3 ", " E "\n\t"
  if (wave == 15) {
    const int off = WS_SLOT + (h * 8 + G) * S_SIZE + S_O + rg * 256 + j * 16;
    unsigned r0, r1, r2, r3, r4, r5, r6, r7, cnt = SPIN_CAP;
    asm volatile(
        "buffer_load_dwordx4 v[104:107], %[off], %[rs], 0 offen sc1\n\t"
        "buffer_load_dwordx4 v[108:111], %[off], %[rs], 0 offen offset:128 sc1\n\t"
        "s_sleep 8\n"
        "1:\n\t"
        "buffer_load_dwordx4 v[112:115], %[off], %[rs], 0 offen sc1\n\t"
        "buffer_load_dwordx4 v[116:119], %[off], %[rs], 0 offen offset:128 sc1\n\t"
        "s_waitcnt vmcnt(2)\n\t"
        CHK4("%0", "%1", "v104", "v105", "v106", "v107") CHK4("%2", "%3", "v108", "v109", "v110", "v111")
        "v_or_b32 %0, %0, %2\n\tv_and_b32 %0, 0x3ff, %0\n\tv_cmp_ne_u32 vcc, 0, %0\n\t"
        "s_cbranch_vccz 2f\n\t"
        "buffer_load_dwordx4 v[104:107], %[off], %[rs], 0 offen sc1\n\t"
        "buffer_load_dwordx4 v[108:111], %[off], %[rs], 0 offen offset:128 sc1\n\t"
        "s_waitcnt vmcnt(2)\n\t"
        CHK4("%0", "%1", "v112", "v113", "v114", "v115") CHK4("%2", "%3", "v116", "v117", "v118", "v119")
        "v_or_b32 %0, %0, %2\n\tv_and_b32 %0, 0x3ff, %0\n\tv_cmp_ne_u32 vcc, 0, %0\n\t"
        "s_cbranch_vccz 3f\n\t"
        "s_sub_u32 %8, %8, 1\n\t"
        "s_cmp_lg_u32 %8, 0\n\t"
        "s_cbranch_scc1 1b\n"
        "3:\n\t"
        MOV4("%0", "%1", "%2", "%3", "v112", "v113", "v114", "v115") MOV4("%4", "%5", "%6", "%7", "v116", "v117", "v118", "v119")
        "s_branch 4f\n"
        "2:\n\t"
        MOV4("%0", "%1", "%2", "%3", "v104", "v105", "v106", "v107") MOV4("%4", "%5", "%6", "%7", "v108", "v109", "v110", "v111")
        "4:"
        : "=&v"(r0), "=&v"(r1), "=&v"(r2), "=&v"(r3), "=&v"(r4), "=&v"(r5), "=&v"(r6), "=&v"(r7), "+s"(cnt)
        : [off] "v"(off), [rs] "s"(rs), [t0] "s"(t0_), [t1] "s"(t1_), [t2] "s"(t2_), [t3] "s"(t3_)
        : "memory", "vcc", "scc", "v104", "v105", "v106", "v107", "v108", "v109", "v110", "v111", "v112", "v113", "v114", "v115", "v116",
          "v117", "v118", "v119");
    if (cnt == 0) ok = false;
    float ls = dec(r3);
    ls += dpp<XOR1>(ls);
    const float M = maxG(ls);
    const float w = __builtin_amdgcn_exp2f(ls - M);
    const float Wt = sumG(w);
    float c0 = tr32(w * dec(r0), w * dec(r4)), c1 = tr32(w * dec(r1), w * dec(r5)), c2 = tr32(w * dec(r2), w * dec(r6));
    c0 = tr16(c0, c0); c1 = tr16(c1, c1); c2 = tr16(c2, c2);
    c0 += dpp<ROR8>(c0); c1 += dpp<ROR8>(c1); c2 += dpp<ROR8>(c2);
    float f = gmix * __builtin_amdgcn_rcpf(Wt);
    if (!__all(ok)) f = __builtin_nanf("");
    if ((lane & 24) == 0) {
      float* dst = lds + L_COEF + (lane >> 5) * 24 + j * 3;
      dst[0] = c0 * f; dst[1] = c1 * f; dst[2] = c2 * f;
    }
    __syncthreads();
  } else {
    __syncthreads();
    const f32x4 cf = *(const f32x4*)cfp;
    f32x2 o[4];
#pragma unroll
    for (int c = 0; c < 4; ++c) o[c] = cf.x * wf[c] + cf.y * wf[4 + c] + (cf.z * wf[8 + c] + cf.w * wf[12 + c]);
    if (lane < 40) {
      *(f32x4*)pdst = f32x4{o[0].x, o[0].y, o[1].x, o[1].y};
      *(f32x4*)(pdst + 160) = f32x4{o[2].x, o[2].y, o[3].x, o[3].y};
    }
  }
  __syncthreads();
  if (wave < 5) {
    f32x4 a = *(const f32x4*)psrc + *(const f32x4*)(psrc + 4 * PS);
    const f32x4 c = *(const f32x4*)(psrc + 8 * PS);
    const f32x4 d = *(const f32x4*)(psrc + 12 * PS);
    const f32x4 z4 = {0.f, 0.f, 0.f, 0.f};
    a += c + ((tid & 3) < 3 ? d : z4);
    a.x += dpp<XOR1>(a.x); a.y += dpp<XOR1>(a.y); a.z += dpp<XOR1>(a.z); a.w += dpp<XOR1>(a.w);
    a.x += dpp<XOR2>(a.x); a.y += dpp<XOR2>(a.y); a.z += dpp<XOR2>(a.z); a.w += dpp<XOR2>(a.w);
    STP(a.x, a.y, a.z, a.w, f_off);
  }
  if (b < 40) {
    const int rh = b / 20, pc = b % 20;
    if (wave >= 8) {
      const int prod = ((wave - 8) * 16 + (lane >> 2)) * 2 + rh;
      const int off = WS_SLOT + prod * S_SIZE + S_FIN + pc * 64 + (lane & 3) * 16;
      unsigned r0, r1, r2, r3, cnt = SPIN_CAP;
      asm volatile(
          "buffer_load_dwordx4 v[112:115], %[off], %[rs], 0 offen sc1\n\t"
          "s_sleep 8\n"
          "1:\n\t"
          "buffer_load_dwordx4 v[116:119], %[off], %[rs], 0 offen sc1\n\t"
          "s_waitcnt vmcnt(1)\n\t"
          CHK4("%0", "%1", "v112", "v113", "v114", "v115")
          "v_and_b32 %0, 0x3ff, %0\n\tv_cmp_ne_u32 vcc, 0, %0\n\t"
          "s_cbranch_vccz 2f\n\t"
          "buffer_load_dwordx4 v[112:115], %[off], %[rs], 0 offen sc1\n\t"
          "s_waitcnt vmcnt(1)\n\t"
          CHK4("%0", "%1", "v116", "v117", "v118", "v119")
          "v_and_b32 %0, 0x3ff, %0\n\tv_cmp_ne_u32 vcc, 0, %0\n\t"
          "s_cbranch_vccz 3f\n\t"
          "s_sub_u32 %4, %4, 1\n\t"
          "s_cmp_lg_u32 %4, 0\n\t"
          "s_cbranch_scc1 1b\n"
          "3:\n\t"
          MOV4("%0", "%1", "%2", "%3", "v116", "v117", "v118", "v119")
          "s_branch 4f\n"
          "2:\n\t"
          MOV4("%0", "%1", "%2", "%3", "v112", "v113", "v114", "v115")
          "4:"
          : "=&v"(r0), "=&v"(r1), "=&v"(r2), "=&v"(r3), "+s"(cnt)
          : [off] "v"(off), [rs] "s"(rs), [t0] "s"(t0_), [t1] "s"(t1_), [t2] "s"(t2_), [t3] "s"(t3_)
          : "memory", "vcc", "scc", "v112", "v113", "v114", "v115", "v116", "v117", "v118", "v119");
      const float u0 = tr32(dec(r0), dec(r2)), u1 = tr32(dec(r1), dec(r3));
      float v = tr16(u0, u1);
      v += dpp<ROR8>(v); v += dpp<ROR4>(v);
      if (cnt == 0) v = __builtin_nanf("");
      if ((lane & 12) == 0) lds[L_PART + (wave - 8) * 16 + (lane & 3) * 4 + (lane >> 4)] = v;
      __syncthreads();
    } else {
      __syncthreads();
      if (wave == 6 && lane < 16) {
        float a = lds[L_PART + lane];
#pragma unroll
        for (int w = 1; w < 8; ++w) a += lds[L_PART + w * 16 + lane];
        const __bf16 rr = (__bf16)a;
        out[rh * 320 + pc * 16 + lane] = __builtin_bit_cast(u16, rr);
        if (b == 0 && lane == 0)
          __hip_atomic_store((GAS u64*)(ws + WS_EPOCH), ((u64)thi << 32) | tlo, __ATOMIC_RELAXED, __HIP_MEMORY_SCOPE_AGENT);
      }
    }
  }
}

extern "C" void kernel_launch(void* const* d_in, const int* in_sizes, int n_in, void* d_out, int out_size, void* d_ws, size_t ws_size,
                              hipStream_t stream) {
  fused_kernel<<<NWG, NTHR, 0, stream>>>((const u16*)d_in[0], (const u16*)d_in[1], (const u16*)d_in[2], (const u16*)d_in[3], (const u16*)d_in[4],
                                         (const u16*)d_in[6], (const u16*)d_in[7], (const u16*)d_in[8], (const u16*)d_in[9], (u16*)d_out,
                                         (char*)d_ws);
}
```
